```python
import torch

def _softmax(x, axis=-1):
    return torch.softmax(x, dim=axis)
def _gelu(x):
    return torch.nn.functional.gelu(x, approximate="tanh")
def _silu(x):
    return torch.nn.functional.silu(x)
def _sigmoid(x):
    return torch.sigmoid(x)
def _layernorm(x, axis=-1, eps=1e-5):
    m = x.mean(dim=axis, keepdim=True)
    v = x.var(dim=axis, keepdim=True, unbiased=False)
    return (x - m) / torch.sqrt(v + eps)
def _scatter_add(tgt, idx, src, axis=0):
    return tgt.clone().index_add_(axis, idx, src)
def _segment_sum(src, seg, n):
    out = torch.zeros((n,) + src.shape[1:], dtype=src.dtype)
    return out.index_add_(0, seg, src)
def _pad(x, pw):
    flat = tuple(v for pair in reversed(pw) for v in pair)
    return torch.nn.functional.pad(x, flat)

kernel_name = 'interleave_layers__ssm_conv_s4d__linear_kernel_hedgehog__prompt'

def setup_inputs():

    _w = (lambda a: (lambda t: t.to(torch.bfloat16) if t.is_floating_point() else t)(torch.as_tensor(a)))
    def _A_setup():
        Bsz, L, D, N = 1, 2048, 512, 128
        return {
            "u": _w((lambda *s: torch.randn(*s, dtype=torch.bfloat16) * 0.05)(Bsz, L, D)),
            "log_A_real": _w((lambda *s: torch.randn(*s, dtype=torch.bfloat16) * 3.2)(D, N) * 0.5),
            "A_imag": _w((lambda *s: torch.randn(*s, dtype=torch.bfloat16) * 0.05)(D, N) * 3.14),
            "C_real": _w((lambda *s: torch.randn(*s, dtype=torch.bfloat16) * 0.05)(D, N) / N ** 0.5),
            "C_imag": _w((lambda *s: torch.randn(*s, dtype=torch.bfloat16) * 0.05)(D, N) / N ** 0.5),
            "log_dt": _w((lambda *s: torch.randn(*s, dtype=torch.bfloat16) * 0.05)(D) * 0.5 - 4.0),
            "D_skip": _w((lambda *s: torch.randn(*s, dtype=torch.bfloat16) * 0.05)(D)),
        }
    _A_in = _A_setup()

    def _B_setup():
        B, S, D, H = 1, 2048, 512, 32
        Dh = D // H
        return {
            "x": _w((lambda *s: torch.randn(*s, dtype=torch.bfloat16) * 0.05)(B, S, D)),
            "wqkv": _w((lambda *s: torch.randn(*s, dtype=torch.bfloat16) * 0.4)(D, 3 * D)),
            "w_phi": _w((lambda *s: torch.randn(*s, dtype=torch.bfloat16) * 0.4)(H, Dh, Dh) * (Dh ** -0.5)),
            "wo": _w((lambda *s: torch.randn(*s, dtype=torch.bfloat16) * 0.05)(D, D)),
        }
    _B_in = _B_setup()

    return {
        "A_u": _A_in["u"],
        "A_log_A_real": _A_in["log_A_real"],
        "A_A_imag": _A_in["A_imag"],
        "A_C_real": _A_in["C_real"],
        "A_C_imag": _A_in["C_imag"],
        "A_log_dt": _A_in["log_dt"],
        "A_D_skip": _A_in["D_skip"],
        "B_x": _B_in["x"],
        "B_wqkv": _B_in["wqkv"],
        "B_w_phi": _B_in["w_phi"],
        "B_wo": _B_in["wo"],
    }

def reference(A_u, A_log_A_real, A_A_imag, A_C_real, A_C_imag, A_log_dt, A_D_skip, B_x, B_wqkv, B_w_phi, B_wo):
    def _A_ref(u, log_A_real, A_imag, C_real, C_imag, log_dt, D_skip):
        L = u.shape[1]
        dt = torch.exp(log_dt)
        A = -torch.exp(log_A_real) + 1j * A_imag
        C = (C_real + 1j * C_imag) * dt[:, None]
        pos = (lambda x, dt: x.to(dt))(torch.arange(L), log_dt.dtype)
        P = torch.exp((A * dt[:, None])[:, :, None] * pos[None, None, :])
        K = 2.0 * torch.einsum("dn,dnl->dl", C, P).real
        L2 = 2 * L
        u_t = (lambda x, *d: x.permute(*d))(u, 0, 2, 1)
        Kf = (lambda x, *a, **k: torch.fft.rfft(x.float(), *a, **k))(K, L2)
        Uf = (lambda x, *a, **k: torch.fft.rfft(x.float(), *a, **k))(u_t, L2)
        y = (lambda x, *a, **k: torch.fft.irfft(x, *a, **k).to(torch.bfloat16))(Uf * Kf[None, :, :], L2)[:, :, :L]
        y = (lambda x, *d: x.permute(*d))(y, 0, 2, 1)
        return y + u * D_skip[None, None, :]

    def _B_ref(x, wqkv, w_phi, wo):
        B, S, D = x.shape
        H = 32; Dh = D // H
        qkv = (lambda x, *d: x.permute(*d))((x @ wqkv).reshape(B, S, 3, H, Dh), 2, 0, 3, 1, 4)
        q, k, v = qkv[0], qkv[1], qkv[2]
        pq = torch.einsum("bhsd,hdf->bhsf", q, w_phi)
        pk = torch.einsum("bhsd,hdf->bhsf", k, w_phi)
        phi_q = torch.concatenate([_softmax(pq, axis=-1), _softmax(-pq, axis=-1)], axis=-1)
        phi_k = torch.concatenate([_softmax(pk, axis=-1), _softmax(-pk, axis=-1)], axis=-1)
        kv = torch.einsum("bhsf,bhsd->bhfd", phi_k, v)
        z  = torch.einsum("bhsf->bhf", phi_k)
        num = torch.einsum("bhqf,bhfd->bhqd", phi_q, kv)
        den = torch.einsum("bhqf,bhf->bhq", phi_q, z)[..., None] + 1e-6
        o = num / den
        return (lambda x, *d: x.permute(*d))(o, 0, 2, 1, 3).reshape(B, S, D) @ wo

    x = A_u
    x = x + _A_ref(_layernorm(x, axis=-1), A_log_A_real, A_A_imag, A_C_real, A_C_imag, A_log_dt, A_D_skip)
    x = x + _B_ref(_layernorm(x, axis=-1), B_wqkv, B_w_phi, B_wo)
    x = x + _A_ref(_layernorm(x, axis=-1), A_log_A_real, A_A_imag, A_C_real, A_C_imag, A_log_dt, A_D_skip)
    x = x + _B_ref(_layernorm(x, axis=-1), B_wqkv, B_w_phi, B_wo)
    return x
```

```cpp
#include <hip/hip_runtime.h>
#include <hip/hip_bf16.h>
#include <stdint.h>

#define LAS __attribute__((address_space(3)))
typedef unsigned v4u __attribute__((ext_vector_type(4)));
typedef unsigned v2u __attribute__((ext_vector_type(2)));
typedef float f4 __attribute__((ext_vector_type(4)));
typedef float f2 __attribute__((ext_vector_type(2)));
typedef short s4 __attribute__((ext_vector_type(4)));
typedef _Float16 h8 __attribute__((ext_vector_type(8)));
typedef _Float16 h2 __attribute__((ext_vector_type(2)));
typedef __bf16 b8 __attribute__((ext_vector_type(8)));
typedef __bf16 b2 __attribute__((ext_vector_type(2)));
typedef LAS char* lptr;

#define PROFILE 0
#define PRE2 18
#define PRE3 9
#define PRE4 14
#define PRE5 10
#define CODE_PF 34000

namespace {

constexpr int WS_EPOCH = 0;
constexpr int WS_G1 = 4096;
constexpr int WS_ST = 65536;
constexpr int WS_SX = 196608;
constexpr int WS_WOF = 262144;
constexpr int WS_XT = 1 << 20;
constexpr int WS_KVP = 9 << 20;
constexpr int KVP_REC = 2176;
constexpr int KVP_LAYER = 16 * 16 * KVP_REC;
constexpr int WS_OB = 12 << 20;
constexpr int WS_DBG = 16 << 20;
constexpr int WS_BYTES = 20 << 20;
constexpr int XT_BYTES = 2 << 20;

constexpr int L_W = 0;
constexpr int L_R = 98304;
constexpr int R_STRIDE = 4176;
constexpr int L_MISC = L_R + 2 * R_STRIDE;
constexpr int L_S = L_MISC + 1536;
constexpr int LDS_BYTES = 163840;
constexpr int L_KIMG = 65536;
constexpr int KROW = 528;
constexpr int KIMG_CH = 81 * KROW;
constexpr int L_TT = 150 * 1024;
constexpr int L_KINV = 160000;
constexpr int L_X0T = 151104;
constexpr int L_KC = L_S;
constexpr int KC_COPY = 4160, KC_CH = 4 * KC_COPY;
constexpr int L_U = L_S + 2 * KC_CH;
constexpr int U_CH = 4608;
constexpr int L_Y = L_S;
constexpr int L_RING = L_S;
constexpr int L_QRAW = L_S + 32768;
constexpr int L_STP = L_S + 49152;
constexpr int L_FLAG = L_S + 49664;
constexpr int L_WST = L_S + 50176;
constexpr int L_XSA = L_S + 51200;
constexpr int L_KVX = L_S;
constexpr int L_RED = L_S + 9216;
constexpr int L_OT = L_S;
constexpr int L_XTILE = L_S + 32768;

static_assert(L_U + 2 * U_CH <= LDS_BYTES, "lds");
static_assert(L_XTILE + 9216 <= LDS_BYTES, "lds");
static_assert(L_XSA + 4096 <= LDS_BYTES && L_X0T + 8 * 1040 <= L_KINV && L_KIMG + 2 * KIMG_CH <= L_X0T && L_RED + 4 * 136 * 32 <= L_QRAW, "lds");

constexpr unsigned SPIN_CAP = 1u << 16;
constexpr unsigned POISON = 0xFFFFFFFFu;

__device__ __forceinline__ float bflo(unsigned d) { return __uint_as_float(d << 16); }
__device__ __forceinline__ float bfhi(unsigned d) { return __uint_as_float(d & 0xFFFF0000u); }
__device__ __forceinline__ unsigned pkbf(float a, float b) { f2 v = {a, b}; return __builtin_bit_cast(unsigned, __builtin_convertvector(v, b2)); }
__device__ __forceinline__ unsigned pkh(float a, float b) { f2 v = {a, b}; return __builtin_bit_cast(unsigned, __builtin_convertvector(v, h2)); }
__device__ __forceinline__ unsigned pkh2(f2 v) { return __builtin_bit_cast(unsigned, __builtin_convertvector(v, h2)); }
__device__ __forceinline__ float rbf(float a) { return bflo(pkbf(a, a)); }
__device__ __forceinline__ unsigned san(unsigned d) { return min(d, 0xFFFFFFFEu); }
typedef unsigned short us2 __attribute__((ext_vector_type(2)));
__device__ __forceinline__ unsigned san16(unsigned d) { const unsigned c = 0xFFFEFFFEu; return __builtin_bit_cast(unsigned, __builtin_elementwise_min(__builtin_bit_cast(us2, d), __builtin_bit_cast(us2, c))); }
__device__ __forceinline__ unsigned mx4(v4u v) { return max(max(v.x, v.y), max(v.z, v.w)); }

__device__ __forceinline__ f4 mfma_bf(v4u a, v4u b, f4 c) { return __builtin_amdgcn_mfma_f32_16x16x32_bf16(__builtin_bit_cast(b8, a), __builtin_bit_cast(b8, b), c, 0, 0, 0); }
__device__ __forceinline__ f4 mfma_h(v4u a, v4u b, f4 c) { return __builtin_amdgcn_mfma_f32_16x16x32_f16(__builtin_bit_cast(h8, a), __builtin_bit_cast(h8, b), c, 0, 0, 0); }
__device__ __forceinline__ f4 mfma16(v2u a, v2u b, f4 c) { return __builtin_amdgcn_mfma_f32_16x16x16bf16_1k(__builtin_bit_cast(s4, a), __builtin_bit_cast(s4, b), c, 0, 0, 0); }

__device__ __forceinline__ f4 zero4() { float z = 0.f; asm volatile("" : "+v"(z)); return f4{z, z, z, z}; }
__device__ __forceinline__ v4u zero4u() { unsigned z = 0u; asm volatile("" : "+v"(z)); return v4u{z, z, z, z}; }
__device__ __forceinline__ v4u lds128(lptr p) { return *(LAS v4u*)p; }
__device__ __forceinline__ v2u lds64(lptr p) { return *(LAS v2u*)p; }
__device__ __forceinline__ f4 ldsf4(lptr p) { return *(LAS f4*)p; }
__device__ __forceinline__ v2u ldstr(lptr p) { return __builtin_bit_cast(v2u, __builtin_amdgcn_ds_read_tr16_b64_v4i16((LAS s4*)p)); }
__device__ __forceinline__ v4u ldstr2(lptr p, int second) { v2u a = ldstr(p), b = ldstr(p + second); return v4u{a.x, a.y, b.x, b.y}; }
template <int OFF> __device__ __forceinline__ v2u dsr64(unsigned addr) { v2u r; asm volatile("ds_read_b64 %0, %1 offset:%2" : "=v"(r) : "v"(addr), "i"(OFF)); return r; }

template <int OFF> __device__ __forceinline__ v4u dsr128(unsigned addr) { v4u r; asm volatile("ds_read_b128 %0, %1 offset:%2" : "=v"(r) : "v"(addr), "i"(OFF)); return r; }
template <int V> struct ic { static constexpr int value = V; };
template <int I, int N> struct SFor { template <class F> static __device__ __forceinline__ void run(F&& f) { f(ic<I>{}); SFor<I + 1, N>::run(f); } };
template <int N> struct SFor<N, N> { template <class F> static __device__ __forceinline__ void run(F&&) {} };

#define BAR() asm volatile("s_waitcnt lgkmcnt(0)\n\ts_barrier" ::: "memory")
#define DRAIN() asm volatile("s_waitcnt vmcnt(0)" ::: "memory")
#define SCHED() __builtin_amdgcn_sched_barrier(0)
constexpr int SC1 = 16;
typedef __amdgpu_buffer_rsrc_t rsrc_t;
__device__ __forceinline__ v4u ld128(rsrc_t r, int off) { return __builtin_amdgcn_raw_buffer_load_b128(r, off, 0, SC1); }
__device__ __forceinline__ v4u ld128s(rsrc_t r, int off, int soff) { return __builtin_amdgcn_raw_buffer_load_b128(r, off, soff, SC1); }
__device__ __forceinline__ unsigned ld32(rsrc_t r, int off) { return __builtin_amdgcn_raw_buffer_load_b32(r, off, 0, SC1); }
__device__ __forceinline__ void st128(rsrc_t r, int off, v4u v) { __builtin_amdgcn_raw_buffer_store_b128(v, r, off, 0, SC1); }
__device__ __forceinline__ void st64(rsrc_t r, int off, v2u v) { __builtin_amdgcn_raw_buffer_store_b64(v, r, off, 0, SC1); }
__device__ __forceinline__ void st32(rsrc_t r, int off, unsigned v) { __builtin_amdgcn_raw_buffer_store_b32(v, r, off, 0, SC1); }
__device__ __forceinline__ void fill128(rsrc_t r, int off) { unsigned p = POISON; asm volatile("" : "+v"(p)); st128(r, off, v4u{p, p, p, p}); }

__device__ __forceinline__ void dma16(const void* g, unsigned lds_) {
  const unsigned lds = __builtin_amdgcn_readfirstlane(lds_);
  unsigned keep;
  asm volatile("s_mov_b32 %0, m0\n\ts_mov_b32 m0, %2\n\ts_nop 0\n\tglobal_load_lds_dwordx4 %1, off\n\ts_mov_b32 m0, %0" : "=&s"(keep) : "v"(g), "s"(lds) : "memory");
}

__device__ __forceinline__ const char* uniform_ptr(const char* p) {
  const unsigned long long a = (unsigned long long)p;
  return (const char*)(((unsigned long long)(unsigned)__builtin_amdgcn_readfirstlane((int)(a >> 32)) << 32) | (unsigned)__builtin_amdgcn_readfirstlane((int)a));
}
__device__ __forceinline__ void dma16s_sc1(const void* sbase, unsigned voff, unsigned lds) {
  unsigned keep;
  asm volatile("s_mov_b32 %0, m0\n\ts_mov_b32 m0, %3\n\ts_nop 0\n\tglobal_load_lds_dwordx4 %1, %2 sc1\n\ts_mov_b32 m0, %0" : "=&s"(keep) : "v"(voff), "s"(sbase), "s"(lds) : "memory");
}
__device__ __forceinline__ float x16add(float a) { auto r = __builtin_amdgcn_permlane16_swap(__float_as_uint(a), __float_as_uint(a), false, false); return __uint_as_float(r[0]) + __uint_as_float(r[1]); }
__device__ __forceinline__ float x32add(float a) { auto r = __builtin_amdgcn_permlane32_swap(__float_as_uint(a), __float_as_uint(a), false, false); return __uint_as_float(r[0]) + __uint_as_float(r[1]); }
__device__ __forceinline__ float x16max(float a) { auto r = __builtin_amdgcn_permlane16_swap(__float_as_uint(a), __float_as_uint(a), false, false); return fmaxf(__uint_as_float(r[0]), __uint_as_float(r[1])); }
__device__ __forceinline__ float x32max(float a) { auto r = __builtin_amdgcn_permlane32_swap(__float_as_uint(a), __float_as_uint(a), false, false); return fmaxf(__uint_as_float(r[0]), __uint_as_float(r[1])); }
template <int CTRL> __device__ __forceinline__ float dpp(float a) { return __uint_as_float(__builtin_amdgcn_update_dpp(0u, __float_as_uint(a), CTRL, 0xf, 0xf, true)); }
__device__ __forceinline__ float row16add(float a) { a += dpp<0x128>(a); a += dpp<0x124>(a); a += dpp<0x122>(a); a += dpp<0x121>(a); return a; }
__device__ __forceinline__ float row16max(float a) { a = fmaxf(a, dpp<0x128>(a)); a = fmaxf(a, dpp<0x124>(a)); a = fmaxf(a, dpp<0x122>(a)); a = fmaxf(a, dpp<0x121>(a)); return a; }
__device__ __forceinline__ float waveadd(float a) { return x32add(x16add(row16add(a))); }

struct cmulr { f2 b, nb; __device__ __forceinline__ cmulr(f2 x) : b(x), nb(f2{-x.y, x.x}) {} __device__ __forceinline__ f2 operator()(f2 a) const { return __builtin_elementwise_fma(a.yy, nb, a.xx * b); } };
__device__ __forceinline__ f2 cexp2(float rho2, float th, float e) {
  float mag = __builtin_amdgcn_exp2f(rho2 * e);
  float fr = __builtin_amdgcn_fractf(th * e);
  return f2{mag * __builtin_amdgcn_cosf(fr), mag * __builtin_amdgcn_sinf(fr)};
}
__device__ __forceinline__ float exp_acc(float x) {
  const float hi = x * 1.4426950216293335f;
  const float lo = fmaf(x, 1.4426950216293335f, -hi) + x * 1.9259629911e-8f;
  const float e = __builtin_amdgcn_exp2f(hi);
  return fmaf(e, lo * 0.6931471805599453f, e);
}

#define STAMP(i) do { } while (0)
#define STAMP0(i) do { } while (0)
#define CSTAMP(i) do { } while (0)
#define RSTAMP(i, v) do { } while (0)

#define FRESH() int t_ = threadIdx.x, w_ = blockIdx.x, wv_ = wv0; unsigned lds_ = (unsigned)(size_t)smem0; asm volatile("" : "+v"(t_), "+s"(w_), "+s"(wv_), "+s"(lds_)); \
  const lptr lds = (lptr)(size_t)lds_; (void)lds; \
  const int t = t_, lane = t & 63, l15 = lane & 15, kq = lane >> 4, w = w_, wv = wv_, xcd = w & 7, idx = w >> 3, rb = xcd * 8 + (idx >> 2), cb = idx & 3, \
            rr = xcd * 2 + (idx >> 4), gg = idx & 15, hh = wv & 1, ph = (wv >> 1) & 1, pme = xcd * 32 + idx; \
  (void)t; (void)l15; (void)kq; (void)rb; (void)cb; (void)rr; (void)gg; (void)hh; (void)ph; (void)pme

__device__ __forceinline__ float ln_sd(float var) { return rbf(__builtin_amdgcn_sqrtf(rbf(rbf(fmaxf(var, 0.f)) + 1e-5f))); }

__device__ __forceinline__ void tr4(unsigned& a0, unsigned& a1, unsigned& a2, unsigned& a3) {
  auto s = __builtin_amdgcn_permlane32_swap(a0, a2, false, false); a0 = s[0]; a2 = s[1];
  auto u = __builtin_amdgcn_permlane32_swap(a1, a3, false, false); a1 = u[0]; a3 = u[1];
  auto v = __builtin_amdgcn_permlane16_swap(a0, a1, false, false); a0 = v[0]; a1 = v[1];
  auto x = __builtin_amdgcn_permlane16_swap(a2, a3, false, false); a2 = x[0]; a3 = x[1];
}
__device__ __forceinline__ float max3(float a, float b, float c) { return __builtin_elementwise_maximum(__builtin_elementwise_maximum(a, b), c); }
__device__ __forceinline__ float min3(float a, float b, float c) { return __builtin_elementwise_minimum(__builtin_elementwise_minimum(a, b), c); }
__device__ __forceinline__ void feature_map(const f4 (&pk)[4], v2u (&p1)[4], v2u (&p2)[4]) {
  constexpr float L2E = 1.4426950408889634f;
  unsigned a[4], b[4];
#pragma unroll
  for (int u = 0; u < 4; ++u) { a[u] = pkbf(pk[u][0], pk[u][1]); b[u] = pkbf(pk[u][2], pk[u][3]); }
  tr4(a[0], a[1], a[2], a[3]);
  tr4(b[0], b[1], b[2], b[3]);
  f2 p[8];
#pragma unroll
  for (int k = 0; k < 4; ++k) { p[2 * k] = f2{bflo(a[k]), bfhi(a[k])}; p[2 * k + 1] = f2{bflo(b[k]), bfhi(b[k])}; }
  const float mx = max3(max3(max3(p[0].x, p[0].y, p[1].x), max3(p[1].y, p[2].x, p[2].y), max3(p[3].x, p[3].y, p[4].x)),
                        max3(max3(p[4].y, p[5].x, p[5].y), p[6].x, p[6].y), max3(p[7].x, p[7].y, p[7].y));
  const float mn = min3(min3(min3(p[0].x, p[0].y, p[1].x), min3(p[1].y, p[2].x, p[2].y), min3(p[3].x, p[3].y, p[4].x)),
                        min3(min3(p[4].y, p[5].x, p[5].y), p[6].x, p[6].y), min3(p[7].x, p[7].y, p[7].y));
  const f2 l2 = {L2E, L2E}, cx = {-mx * L2E, -mx * L2E}, cn = {mn * L2E, mn * L2E};
  f2 e[8], g[8];
#pragma unroll
  for (int k = 0; k < 8; ++k) {
    const f2 x = __builtin_elementwise_fma(p[k], l2, cx), y = __builtin_elementwise_fma(p[k], -l2, cn);
    e[k] = f2{__builtin_amdgcn_exp2f(x.x), __builtin_amdgcn_exp2f(x.y)};
    g[k] = f2{__builtin_amdgcn_exp2f(y.x), __builtin_amdgcn_exp2f(y.y)};
  }
  const f2 se = ((e[0] + e[1]) + (e[2] + e[3])) + ((e[4] + e[5]) + (e[6] + e[7])), sg = ((g[0] + g[1]) + (g[2] + g[3])) + ((g[4] + g[5]) + (g[6] + g[7]));
  const float r1 = __builtin_amdgcn_rcpf(se.x + se.y), r2 = __builtin_amdgcn_rcpf(sg.x + sg.y);
  unsigned P[8], Q[8];
#pragma unroll
  for (int k = 0; k < 8; ++k) {
    const f2 x = e[k] * r1, y = g[k] * r2;
    P[k] = pkbf(x.x, x.y);
    Q[k] = pkbf(y.x, y.y);
  }
  tr4(P[0], P[2], P[4], P[6]);
  tr4(P[1], P[3], P[5], P[7]);
  tr4(Q[0], Q[2], Q[4], Q[6]);
  tr4(Q[1], Q[3], Q[5], Q[7]);
#pragma unroll
  for (int u = 0; u < 4; ++u) { p1[u] = v2u{P[2 * u], P[2 * u + 1]}; p2[u] = v2u{Q[2 * u], Q[2 * u + 1]}; }
}

__device__ __forceinline__ void build_copies(lptr lds, int t) {
#pragma unroll
  for (int it = 0; it < 2; ++it) {
    const int task = t + 512 * it;
    if (it == 0 || task < 520) {
      const int ch = task >= 260, G = task - ch * 260;
      lptr src = lds + L_R + ch * R_STRIDE + G * 16;
      v4u d = lds128(src);
      v2u e = lds64(src + 16);
      lptr dst = lds + L_KC + ch * KC_CH + G * 16;
      *(LAS v4u*)(dst) = d;
      *(LAS v4u*)(dst + KC_COPY) = v4u{__builtin_amdgcn_alignbit(d.y, d.x, 16), __builtin_amdgcn_alignbit(d.z, d.y, 16), __builtin_amdgcn_alignbit(d.w, d.z, 16), __builtin_amdgcn_alignbit(e.x, d.w, 16)};
      *(LAS v4u*)(dst + 2 * KC_COPY) = v4u{d.y, d.z, d.w, e.x};
      *(LAS v4u*)(dst + 3 * KC_COPY) = v4u{__builtin_amdgcn_alignbit(d.z, d.y, 16), __builtin_amdgcn_alignbit(d.w, d.z, 16), __builtin_amdgcn_alignbit(e.x, d.w, 16), __builtin_amdgcn_alignbit(e.y, e.x, 16)};
    }
  }
  if (t >= 448) *(LAS v4u*)(lds + L_U + ((t >> 5) & 1) * U_CH + (t & 31) * 16) = zero4u();
}

template <int TS> struct Conv {
  static constexpr int AM = TS == 0 ? 7 : 6;
  static constexpr int T0 = TS == 0 ? 0 : 1, T1 = TS == 0 ? 3 : 2, T2 = TS == 0 ? 4 : 5, T3 = TS == 0 ? 7 : 6;
  v4u B[AM + 3];
  v2u lo[3], hi[3];
  static constexpr int bidx(int b, int s) { return s >= 2 ? s : ((b & 1) ? AM + 1 + s : s); }
  static constexpr int nb(int b, int a) { return b < 3 ? (a <= AM - 2 ? 1 : 0) + (a < 2 ? 1 : 0) : 0; }
  static constexpr int na(int b, int a) { return (b < 3 || a + 2 <= AM) ? 2 : 0; }
  template <int BI, int A_> __device__ __forceinline__ void step(unsigned pa, unsigned pb, f4 (&acc)[4]) {
    constexpr int n = (AM + 1) * BI + A_, cur = n % 3, nx = (n + 2) % 3;
    constexpr int pa2 = A_ >= 2 ? A_ - 2 : A_ - 2 + AM + 1, pb2 = A_ >= 2 ? BI : BI - 1;
    constexpr int pa1 = A_ >= 1 ? A_ - 1 : AM, pb1 = A_ >= 1 ? BI : BI - 1;
    constexpr int young = (pb2 >= 0 ? nb(pb2, pa2) : 0) + (pb1 >= 0 ? na(pb1, pa1) + nb(pb1, pa1) : (A_ == 1 ? na(0, 0) + nb(0, 0) : 0));
    constexpr int O = 64 * (3 - BI);
    constexpr int W = BI == 0 && A_ == 0 ? 0 : young;
    if constexpr (T0 >= A_) asm volatile("s_waitcnt lgkmcnt(%6)" : "+v"(lo[cur]), "+v"(hi[cur]), "+v"(B[bidx(BI, T3 - A_)]), "+v"(B[bidx(BI, T2 - A_)]), "+v"(B[bidx(BI, T1 - A_)]), "+v"(B[bidx(BI, T0 - A_)]) : "i"(W));
    else if constexpr (T1 >= A_) asm volatile("s_waitcnt lgkmcnt(%5)" : "+v"(lo[cur]), "+v"(hi[cur]), "+v"(B[bidx(BI, T3 - A_)]), "+v"(B[bidx(BI, T2 - A_)]), "+v"(B[bidx(BI, T1 - A_)]) : "i"(W));
    else if constexpr (T2 >= A_) asm volatile("s_waitcnt lgkmcnt(%4)" : "+v"(lo[cur]), "+v"(hi[cur]), "+v"(B[bidx(BI, T3 - A_)]), "+v"(B[bidx(BI, T2 - A_)]) : "i"(W));
    else asm volatile("s_waitcnt lgkmcnt(%3)" : "+v"(lo[cur]), "+v"(hi[cur]), "+v"(B[bidx(BI, T3 - A_)]) : "i"(W));
    SCHED();
    const v4u A = v4u{lo[cur].x, lo[cur].y, hi[cur].x, hi[cur].y};
    acc[3] = mfma_h(A, B[bidx(BI, T3 - A_)], acc[3]);
    if constexpr (na(BI, A_) != 0) {
      constexpr int a2 = (A_ + 2) % (AM + 1), O2 = A_ + 2 <= AM ? O : O - 64;
      lo[nx] = dsr64<512 * (7 - a2) + O2>(pa);
      hi[nx] = dsr64<512 * (7 - a2) + O2 + 8>(pa);
    }
    SCHED();
    if constexpr (T2 >= A_) acc[2] = mfma_h(A, B[bidx(BI, T2 - A_)], acc[2]);
    if constexpr (BI < 3 && A_ < 2) B[bidx(BI + 1, A_)] = dsr128<512 * A_ + O - 64>(pb);
    SCHED();
    if constexpr (T1 >= A_) acc[1] = mfma_h(A, B[bidx(BI, T1 - A_)], acc[1]);
    if constexpr (T0 >= A_) acc[0] = mfma_h(A, B[bidx(BI, T0 - A_)], acc[0]);
    SCHED();
    if constexpr (BI < 3 && A_ <= AM - 2) B[AM - A_] = dsr128<512 * (AM - A_) + O - 64>(pb);
    SCHED();
  }
  __device__ __forceinline__ void run(unsigned pa, unsigned pb, f4 (&acc)[4]) {
    SFor<0, AM + 1>::run([&](auto k) { constexpr int s = decltype(k)::value; B[s] = dsr128<512 * s + 192>(pb); });
    lo[0] = dsr64<512 * 7 + 192>(pa); hi[0] = dsr64<512 * 7 + 192 + 8>(pa);
    lo[1] = dsr64<512 * 6 + 192>(pa); hi[1] = dsr64<512 * 6 + 192 + 8>(pa);
    SFor<0, 4>::run([&](auto kb) { SFor<0, AM + 1>::run([&](auto ka) { step<decltype(kb)::value, decltype(ka)::value>(pa, pb, acc); }); });
  }
};
template <int TS> __device__ __forceinline__ void conv_all(unsigned pa, unsigned pb, f4 (&acc)[4]) { Conv<TS> c; c.run(pa, pb, acc); }

}

__global__ void __launch_bounds__(512, 2)
interleave_layers_kernel(const unsigned short* A_u, const unsigned short* logA, const unsigned short* Aim, const unsigned short* Cre,
                         const unsigned short* Cim, const unsigned short* logdt, const unsigned short* Dsk, const unsigned short* wqkv,
                         const unsigned short* wphi, const unsigned short* wo, unsigned short* out, char* ws) {
  extern __shared__ __attribute__((aligned(16))) char smem[];
  const lptr smem0 = (lptr)smem;
  const int wv0 = __builtin_amdgcn_readfirstlane(threadIdx.x >> 6);
  const rsrc_t rs = __builtin_amdgcn_make_buffer_rsrc(ws, 0, WS_BYTES, 0x00020000);
  unsigned tag, dsk;
  float kinv0, kinv1;
  v4u g1a, g1b, x0r;
  {
    FRESH();
    STAMP0(0);
    CSTAMP(60);
    if (wv0 >= 4) __builtin_amdgcn_s_setprio(1);
    const v4u xrow = *(const v4u*)(A_u + (size_t)(8 * w + wv) * 512 + lane * 8);
    const int krole = wv >> 2, kch = (wv >> 1) & 1, kn = (wv & 1) * 64 + lane;
    const int chn = 2 * pme + kch;
    const unsigned short pla = logA[chn * 128 + kn], pai = Aim[chn * 128 + kn];
    const unsigned short pcr = Cre[chn * 128 + kn], pci = Cim[chn * 128 + kn], pcr2 = Cre[chn * 128 + (kn ^ 64)], pci2 = Cim[chn * 128 + (kn ^ 64)];
    const unsigned short pdt = logdt[chn];
    const unsigned dskv = ((const unsigned*)Dsk)[pme];
    const unsigned ep = ld32(rs, WS_EPOCH);
    SCHED();
    __builtin_amdgcn_s_barrier();
    SCHED();
    fill128(rs, WS_XT + 1 * XT_BYTES + pme * 8192 + t * 16);
    unsigned wpf, pf0;
    unsigned short wog[8], wpg[4];
    {
      const int fr = 2 * w + (wv & 1), nt = fr >> 4, s = fr & 15;
      const unsigned short* p = wo + (size_t)(32 * s + 8 * kq) * 512 + 16 * nt + l15;
#pragma unroll
      for (int j = 0; j < 8; ++j) wog[j] = p[j * 512];
      const unsigned short* pp = wphi + (2 * gg + hh) * 256 + (4 * kq) * 16 + l15;
#pragma unroll
      for (int j = 0; j < 4; ++j) wpg[j] = pp[16 * j];
      wpf = ((const unsigned*)wqkv)[(idx * 384 + min(t, 383)) * 32];
      unsigned long long pc;
      asm volatile("s_getpc_b64 %0" : "=s"(pc));
      pf0 = *(const unsigned*)((const char*)(pc & ~127ull) + min(t * 128, CODE_PF - 128));
    }
    SCHED();

    unsigned msd;
    v4u wor;
    *(LAS v4u*)(lds + L_X0T + wv * 1040 + lane * 16) = xrow;
    {
      float v[8];
#pragma unroll
      for (int j = 0; j < 4; ++j) { v[2 * j] = bflo(xrow[j]); v[2 * j + 1] = bfhi(xrow[j]); }
      const float mean = waveadd(((v[0] + v[1]) + (v[2] + v[3])) + ((v[4] + v[5]) + (v[6] + v[7]))) * (1.f / 512.f);
      float ss = 0.f;
#pragma unroll
      for (int j = 0; j < 8; ++j) { const float d = v[j] - mean; ss = fmaf(d, d, ss); }
      msd = pkbf(mean, ln_sd(waveadd(ss) * (1.f / 512.f)));
    }
    BAR();
    {
      lptr p = lds + L_X0T + (t & 1) * 4 * 1040 + (t >> 1) * 4;
      st128(rs, WS_XT + (t >> 1) * 8192 + (8 * w + 4 * (t & 1)) * 4, v4u{*(LAS unsigned*)p, *(LAS unsigned*)(p + 1040), *(LAS unsigned*)(p + 2080), *(LAS unsigned*)(p + 3120)});
    }
    STAMP0(1);

    {
      const float dt = rbf(exp_acc(bflo(pdt)));
      const float are = -rbf(exp_acc(bflo(pla)));
      const float rho2 = (are * dt) * 1.4426950408889634f;
      const float th = (bflo(pai) * dt) * 0.15915494309189535f;
      lptr img = lds + L_KIMG + kch * KIMG_CH + kn * 4;
      if (krole == 0) {
        lptr yb = img + 41 * KROW;
        f2 y[16], z1[8], z2[8], z3[8];
        y[0] = f2{1.f, 0.f};
        y[1] = cexp2(rho2, th, 1.f);
        z1[0] = cexp2(rho2, th, 16.f); z2[0] = cexp2(rho2, th, 32.f); z3[0] = cexp2(rho2, th, 64.f);
        const cmulr m1(y[1]), m16(z1[0]), m32(z2[0]), m64(z3[0]);
        y[2] = m1(y[1]);
        const cmulr m2(y[2]);
        y[3] = m1(y[2]);
        y[4] = m2(y[2]);
        const cmulr m4(y[4]);
        y[5] = m4(y[1]); y[6] = m4(y[2]); y[7] = m4(y[3]); y[8] = m4(y[4]);
        const cmulr m8(y[8]);
#pragma unroll
        for (int j = 9; j < 16; ++j) y[j] = m8(y[j - 8]);
#pragma unroll
        for (int k = 1; k < 8; ++k) z1[k] = m16(y[2 * k]);
#pragma unroll
        for (int j = 0; j < 16; ++j) *(LAS unsigned*)(yb + j * KROW) = pkh2(y[j]);
#pragma unroll
        for (int k = 1; k < 8; ++k) z2[k] = m32(k < 4 ? y[4 * k] : z1[2 * (k - 4)]);
#pragma unroll
        for (int k = 0; k < 8; ++k) *(LAS unsigned*)(yb + (16 + k) * KROW) = pkh2(z1[k]);
#pragma unroll
        for (int k = 1; k < 8; ++k) z3[k] = m64(k < 2 ? y[8 * k] : k < 4 ? z1[4 * (k - 2)] : z2[2 * (k - 4)]);
#pragma unroll
        for (int k = 0; k < 8; ++k) *(LAS unsigned*)(yb + (24 + k) * KROW) = pkh2(z2[k]);
#pragma unroll
        for (int k = 0; k < 8; ++k) *(LAS unsigned*)(yb + (32 + k) * KROW) = pkh2(z3[k]);
      } else {
        const float cr = bflo(pcr) * dt, ci = bflo(pci) * dt, tn = -th;
        float mx = fmaxf(fmaxf(fabsf(cr), fabsf(ci)), fmaxf(fabsf(bflo(pcr2) * dt), fabsf(bflo(pci2) * dt)));
        mx = x32max(x16max(row16max(mx)));
        int ex = 4 - __builtin_amdgcn_frexp_expf(mx);
        ex = min(max(ex, -120), 120);
        const float sc2 = __builtin_amdgcn_ldexpf(2.f, ex);
        if ((wv & 1) == 0 && lane == 0) *(LAS float*)(lds + L_KINV + kch * 4) = __builtin_amdgcn_ldexpf(1.f, -ex);
        const cmulr mc(f2{cr * sc2, -ci * sc2});
        const f2 e128 = cexp2(rho2, tn, 128.f);
        const cmulr st[5] = {cmulr(cexp2(rho2, tn, 16.f)), cmulr(cexp2(rho2, tn, 16.f)), cmulr(cexp2(rho2, tn, 32.f)), cmulr(cexp2(rho2, tn, 64.f)), cmulr(e128)};
        f2 x[5] = {mc.b, mc(e128), mc(cexp2(rho2, tn, 256.f)), mc(cexp2(rho2, tn, 512.f)), mc(cexp2(rho2, tn, 1024.f))};
#pragma unroll
        for (int i = 0; i < 8; ++i) {
          f2 tm[5];
#pragma unroll
          for (int s = 0; s < 5; ++s) tm[s] = x[s].xx * st[s].b;
#pragma unroll
          for (int s = 0; s < 5; ++s) *(LAS unsigned*)(img + (8 * s + i) * KROW) = pkh2(x[s]);
#pragma unroll
          for (int s = 0; s < 5; ++s) x[s] = __builtin_elementwise_fma(x[s].yy, st[s].nb, tm[s]);
          SCHED();
        }
        *(LAS unsigned*)(img + 40 * KROW) = pkh2(x[4]);
      }
    }
    __builtin_amdgcn_s_setprio(0);
    STAMP0(4);
    DRAIN();
    tag = __builtin_amdgcn_readfirstlane(ep) + 1u;
    dsk = __builtin_amdgcn_readfirstlane(dskv);
    STAMP0(5);
    asm volatile("" :: "v"(wpf), "v"(pf0));
    if (lane == 0) st64(rs, WS_G1 + (8 * w + wv) * 8, v2u{msd, tag});
    wor = v4u{wog[0] | ((unsigned)wog[1] << 16), wog[2] | ((unsigned)wog[3] << 16), wog[4] | ((unsigned)wog[5] << 16), wog[6] | ((unsigned)wog[7] << 16)};
    if (wv < 2) st128(rs, WS_WOF + (2 * w + wv) * 1024 + lane * 16, wor);
#define W_PIECE(q) do { const int part_ = (q) >> 5, k_ = ((q) & 31) * 16 + (lane >> 2), c_ = (lane & 3) ^ (((k_ >> 3) & 1) << 1); \
      dma16(wqkv + (size_t)k_ * 1536 + part_ * 512 + gg * 32 + c_ * 8, (unsigned)(L_W + (q) * 1024)); } while (0)
#pragma unroll
    for (int i = 0; i < 8; ++i) W_PIECE(wv * 8 + i);
    BAR();
    STAMP0(2);
    {
      const int ch = wv >> 2, s = wv & 3;
      const int xrow_ = s == 0 ? l15 : 8 + 8 * s + min(l15, s == 3 ? 8 : 7);
      const int e = l15 << s;
      const int yrow = 41 + (e < 16 ? e : e < 32 ? 16 + ((e - 16) >> 1) : e < 64 ? 24 + ((e - 32) >> 2) : 32 + ((e - 64) >> 3));
      lptr pa = lds + L_KIMG + ch * KIMG_CH + xrow_ * KROW + kq * 16, pb = lds + L_KIMG + ch * KIMG_CH + yrow * KROW + kq * 16;
      v4u fa[8], fb[8];
#pragma unroll
      for (int ks = 0; ks < 8; ++ks) { fa[ks] = lds128(pa + ks * 64); fb[ks] = lds128(pb + ks * 64); }
      SCHED();
      f4 acc = {0.f, 0.f, 0.f, 0.f}, acc2 = acc;
#pragma unroll
      for (int ks = 0; ks < 8; ks += 2) { acc = mfma_h(fa[ks], fb[ks], acc); acc2 = mfma_h(fa[ks + 1], fb[ks + 1], acc2); }
      acc += acc2;
#pragma unroll
      for (int r = 0; r < 4; ++r) {
        const int i = 4 * kq + r;
        const int ix = s == 0 ? 16 * i + l15 : 256 + (s - 1) * 128 + 16 * i + l15;
        const bool ok = s == 0 || i < 8 || (s == 3 && i == 8 && l15 == 0);
        if (ok) *(LAS _Float16*)(lds + L_TT + ch * 1296 + ix * 2) = (_Float16)acc[r];
      }
    }
    BAR();
    STAMP0(6);
    g1a = ld128(rs, WS_G1 + t * 32);
    g1b = ld128(rs, WS_G1 + t * 32 + 16);
    SCHED();
    kinv0 = __uint_as_float(__builtin_amdgcn_readfirstlane(*(LAS unsigned*)(lds + L_KINV)));
    kinv1 = __uint_as_float(__builtin_amdgcn_readfirstlane(*(LAS unsigned*)(lds + L_KINV + 4)));
    {
      const int ch = t >> 8, tt = t & 255;
      unsigned h[12];
#pragma unroll
      for (int j = 0; j < 12; j += 2) {
        const int lag0 = 2047 - 8 * tt - j, lag1 = lag0 - 1;
        const unsigned pb = pkbf((float)lag0, (float)lag1);
        const int i0 = lag0 < 256 ? max(lag0, 0) : (int)(pb & 0xFFFFu) - 17024, i1 = lag1 < 256 ? max(lag1, 0) : (int)(pb >> 16) - 17024;
        h[j] = *(LAS unsigned short*)(lds + L_TT + ch * 1296 + i0 * 2);
        h[j + 1] = *(LAS unsigned short*)(lds + L_TT + ch * 1296 + i1 * 2);
      }
      const v4u d = v4u{h[0] | (h[1] << 16), h[2] | (h[3] << 16), h[4] | (h[5] << 16), h[6] | (h[7] << 16)};
      const v2u e = tt == 255 ? v2u{0u, 0u} : v2u{h[8] | (h[9] << 16), h[10] | (h[11] << 16)};
      {
        unsigned n = 0;
        while (!__all(g1a.y == tag && g1a.w == tag && g1b.y == tag && g1b.w == tag) && n < SPIN_CAP) {
          ++n;
          asm volatile("" ::: "memory");
          g1a = ld128(rs, WS_G1 + t * 32);
          g1b = ld128(rs, WS_G1 + t * 32 + 16);
        }
        RSTAMP(8 + 16, n);
        asm volatile("" ::: "memory");
        x0r = ld128(rs, WS_XT + pme * 8192 + t * 16);
        SCHED();
      }
      *(LAS v4u*)(lds + L_R + ch * R_STRIDE + 32 + tt * 16) = d;
      if (tt < 5) *(LAS v4u*)(lds + L_R + ch * R_STRIDE + (tt < 2 ? tt * 16 : 4096 + tt * 16)) = zero4u();
      lptr dst = lds + L_KC + ch * KC_CH + (tt + 2) * 16;
      *(LAS v4u*)(dst) = d;
      *(LAS v4u*)(dst + KC_COPY) = v4u{__builtin_amdgcn_alignbit(d.y, d.x, 16), __builtin_amdgcn_alignbit(d.z, d.y, 16), __builtin_amdgcn_alignbit(d.w, d.z, 16), __builtin_amdgcn_alignbit(e.x, d.w, 16)};
      *(LAS v4u*)(dst + 2 * KC_COPY) = v4u{d.y, d.z, d.w, e.x};
      *(LAS v4u*)(dst + 3 * KC_COPY) = v4u{__builtin_amdgcn_alignbit(d.z, d.y, 16), __builtin_amdgcn_alignbit(d.w, d.z, 16), __builtin_amdgcn_alignbit(e.x, d.w, 16), __builtin_amdgcn_alignbit(e.y, e.x, 16)};
      if (tt < 8) *(LAS v4u*)(lds + L_KC + ch * KC_CH + (tt >> 1) * KC_COPY + (258 + (tt & 1)) * 16) = zero4u();
      if (t >= 448) *(LAS v4u*)(lds + L_U + ((t >> 5) & 1) * U_CH + (t & 31) * 16) = zero4u();
    }
    if (wv < 2) *(LAS v2u*)(lds + L_MISC + 512 + wv * 512 + lane * 8) = v2u{wpg[0] | ((unsigned)wpg[1] << 16), wpg[2] | ((unsigned)wpg[3] << 16)};
    STAMP0(3);
  }

  v4u xr, tD;
  auto conv_prep = [&](const float (&m)[4], const float (&rinv)[4]) {
    FRESH();
    const float d0 = bflo(dsk), d1 = bfhi(dsk);
    float u0[4], u1[4];
    unsigned dd[4], uu[4];
#pragma unroll
    for (int p = 0; p < 4; ++p) { const unsigned xp = xr[p]; dd[p] = pkbf(bflo(xp) - m[p], bfhi(xp) - m[p]); }
    SCHED();
#pragma unroll
    for (int p = 0; p < 4; ++p) uu[p] = pkbf(bflo(dd[p]) * rinv[p], bfhi(dd[p]) * rinv[p]);
    SCHED();
#pragma unroll
    for (int p = 0; p < 4; ++p) { u0[p] = bflo(uu[p]); u1[p] = bfhi(uu[p]); tD[p] = pkbf(u0[p] * d0, u1[p] * d1); }
    if (t == 0) *(LAS v4u*)(lds + L_FLAG) = zero4u();
    *(LAS v2u*)(lds + L_U + 512 + t * 8) = v2u{pkh(u0[0], u0[1]), pkh(u0[2], u0[3])};
    *(LAS v2u*)(lds + L_U + U_CH + 512 + t * 8) = v2u{pkh(u1[0], u1[1]), pkh(u1[2], u1[3])};
  };
  {
    FRESH();
    float m[4], rinv[4];
    const v4u ga = g1a, gb = g1b;
    xr = x0r;
    m[0] = bflo(ga.x); m[1] = bflo(ga.z); m[2] = bflo(gb.x); m[3] = bflo(gb.z);
    rinv[0] = __builtin_amdgcn_rcpf(bfhi(ga.x)); rinv[1] = __builtin_amdgcn_rcpf(bfhi(ga.z));
    rinv[2] = __builtin_amdgcn_rcpf(bfhi(gb.x)); rinv[3] = __builtin_amdgcn_rcpf(bfhi(gb.z));
    STAMP(8 + 0);
    conv_prep(m, rinv);
#pragma unroll
    for (int i = 0; i < 4; ++i) W_PIECE(64 + wv * 4 + i);
  }

#pragma unroll 1
  for (int layer = 0; layer < 2; ++layer) {
    const int stamp_off = layer * 24;
    (void)stamp_off;
    BAR();
    STAMP(8 + 1);
    {
      FRESH();
      if (layer == 0 && w == 0 && t == 0) { unsigned a = tag; asm volatile("" : "+s"(a)); st32(rs, WS_EPOCH, a); }
      if (t < KVP_REC / 16) fill128(rs, WS_KVP + layer * KVP_LAYER + (gg * 16 + rr) * KVP_REC + t * 16);
      fill128(rs, WS_OB + layer * XT_BYTES + ((rr * 8 + wv) * 16 + gg) * 1024 + lane * 16);
      if (t >= 448 && t < 450) fill128(rs, WS_SX + layer * 8192 + rr * 512 + gg * 32 + (t & 1) * 16);
    }
    {
      FRESH();
      const int c = wv >> 2, ts = (wv >> 1) & 1, bs = wv & 1;
      const unsigned pa = (unsigned)(size_t)lds + L_KC + c * KC_CH + (3 - (l15 & 3)) * KC_COPY + 2 * (2044 - 4 * (l15 >> 2) + 8 * kq) - 3584 - 64 * (4 * bs + 3);
      const unsigned pb = (unsigned)(size_t)lds + L_U + c * U_CH + 2 * (240 + 16 * l15 + 8 * kq) - 64 * (4 * bs + 3);
      f4 acc[4];
#pragma unroll
      for (int i = 0; i < 4; ++i) acc[i] = f4{0.f, 0.f, 0.f, 0.f};
      if (ts == 0) conv_all<0>(pa, pb, acc);
      else conv_all<1>(pa, pb, acc);
      STAMP(8 + 17);
      DRAIN();
      BAR();
      lptr py = lds + L_Y + (c * 2 + bs) * 8192 + ((4 * l15 + kq) ^ ((l15 >> 1) & 3)) * 16;
      if (ts == 0) {
        *(LAS f4*)(py + 0 * 1024) = acc[0]; *(LAS f4*)(py + 3 * 1024) = acc[1]; *(LAS f4*)(py + 4 * 1024) = acc[2]; *(LAS f4*)(py + 7 * 1024) = acc[3];
      } else {
        *(LAS f4*)(py + 1 * 1024) = acc[0]; *(LAS f4*)(py + 2 * 1024) = acc[1]; *(LAS f4*)(py + 5 * 1024) = acc[2]; *(LAS f4*)(py + 6 * 1024) = acc[3];
      }
    }
    BAR();
    STAMP(8 + 2);
    {
      FRESH();
      lptr py = lds + L_Y + (t ^ ((t >> 3) & 3)) * 16;
      const f4 ya = ldsf4(py), yb_ = ldsf4(py + 8192), yc = ldsf4(py + 16384), yd = ldsf4(py + 24576);
      f4 ya_ = ya, yd_ = yd;
      asm volatile("s_waitcnt lgkmcnt(0)" : "+v"(ya_), "+v"(yd_) :: "memory");
      if (lane == 0) __hip_atomic_fetch_add((LAS unsigned*)(lds + L_FLAG + 8), 1u, __ATOMIC_RELAXED, __HIP_MEMORY_SCOPE_WORKGROUP);
      const f4 y0 = (ya_ + yb_) * kinv0, y1 = (yc + yd_) * kinv1;
      v4u o;
#pragma unroll
      for (int p = 0; p < 4; ++p) {
        const unsigned yb = pkbf(y0[p], y1[p]), xp = xr[p], tp = tD[p];
        const unsigned a = pkbf(bflo(yb) + bflo(tp), bfhi(yb) + bfhi(tp));
        o[p] = pkbf(bflo(xp) + bflo(a), bfhi(xp) + bfhi(a));
      }
      const unsigned c0a = __builtin_amdgcn_perm(o.y, o.x, 0x05040100), c0b = __builtin_amdgcn_perm(o.w, o.z, 0x05040100);
      const unsigned c1a = __builtin_amdgcn_perm(o.y, o.x, 0x07060302), c1b = __builtin_amdgcn_perm(o.w, o.z, 0x07060302);
      const bool odd = t & 1;
      const unsigned ra = __builtin_amdgcn_update_dpp(0u, odd ? c0a : c1a, 0xB1, 0xf, 0xf, true), rb2 = __builtin_amdgcn_update_dpp(0u, odd ? c0b : c1b, 0xB1, 0xf, 0xf, true);
      const v4u oc = odd ? v4u{ra, rb2, c1a, c1b} : v4u{c0a, c0b, ra, rb2};
      st128(rs, WS_XT + (layer * 2 + 1) * XT_BYTES + (2 * pme + (t & 1)) * 4096 + (t >> 1) * 16, v4u{san16(oc.x), san16(oc.y), san16(oc.z), san16(oc.w)});
    }
    STAMP(8 + 3);
    if (layer == 0) {
      FRESH();
      fill128(rs, WS_XT + 2 * XT_BYTES + (cb * 64 + (t >> 3)) * 8192 + (rb * 32 + (t & 7) * 4) * 4);
      if (t < 8) fill128(rs, WS_ST + (cb * 2048 + rb * 32) * 4 + t * 16);
      fill128(rs, WS_XT + 3 * XT_BYTES + pme * 8192 + t * 16);
      if (wv < 6) {
        const int part = wv >> 1;
        lptr p = lds + L_W + part * 32768 + (8 * kq + (l15 >> 2)) * 64 + ((hh ^ (kq & 1)) * 32) + (lane & 3) * 8;
        const v4u ones = v4u{0x3F803F80u, 0x3F803F80u, 0x3F803F80u, 0x3F803F80u};
        f4 cs = {0.f, 0.f, 0.f, 0.f};
#pragma unroll
        for (int s0 = 0; s0 < 16; s0 += 8) {
          v4u fw[8];
#pragma unroll
          for (int s = 0; s < 8; ++s) fw[s] = ldstr2(p + (s0 + s) * 2048, 256);
          SCHED();
#pragma unroll
          for (int s = 0; s < 8; ++s) cs = mfma_bf(fw[s], ones, cs);
        }
        if (l15 == 0) *(LAS f4*)(lds + L_MISC + (part * 32 + hh * 16 + 4 * kq) * 4) = cs;
      }
    }

    const int xt1 = WS_XT + (layer * 2 + 1) * XT_BYTES;
    v2u phq1[4], phq2[4];
    v4u wof[16];
    v4u xsb;
    {
      FRESH();
      if (layer == 0) __builtin_amdgcn_s_sleep(PRE2 > 8 ? PRE2 - 8 : 0);
      else __builtin_amdgcn_s_sleep(PRE2);
      for (unsigned sp = 0; __hip_atomic_load((LAS unsigned*)(lds + L_FLAG + 8), __ATOMIC_RELAXED, __HIP_MEMORY_SCOPE_WORKGROUP) < 8u && sp < (1u << 20); ++sp) __builtin_amdgcn_s_sleep(1);
      asm volatile("" ::: "memory");
      const char* xg = uniform_ptr(ws + xt1 + rr * 256);
      unsigned vo[4];
#pragma unroll
      for (int i = 0; i < 4; ++i) { const int kk = 4 * ((wv & 3) * 4 + i) + kq, f = (kk & 3) | (((kk >> 3) & 1) << 2); vo[i] = kk * 4096 + ((l15 ^ (f << 1)) * 16); }
      if (wv >= 4) {
        const unsigned l1 = L_RING + 16384 + (wv & 3) * 4096;
        const char* xg1 = xg + 64 * 4096;
        const char* xsp = ws + xt1 + (size_t)t * 4096 + (rr * 128 + 8 * gg) * 2;
        unsigned keep;
        asm volatile("global_load_dwordx4 %0, %2, off sc1\n\ts_mov_b32 %1, m0\n\t"
                     "s_mov_b32 m0, %8\n\ts_nop 0\n\tglobal_load_lds_dwordx4 %3, %7 sc1\n\ts_add_u32 m0, m0, 0x400\n\ts_nop 0\n\tglobal_load_lds_dwordx4 %4, %7 sc1\n\t"
                     "s_add_u32 m0, m0, 0x400\n\ts_nop 0\n\tglobal_load_lds_dwordx4 %5, %7 sc1\n\ts_add_u32 m0, m0, 0x400\n\ts_nop 0\n\tglobal_load_lds_dwordx4 %6, %7 sc1\n\t"
                     "s_mov_b32 m0, %1\n\ts_waitcnt vmcnt(4)"
                     : "=&v"(xsb), "=&s"(keep) : "v"(xsp), "v"(vo[0]), "v"(vo[1]), "v"(vo[2]), "v"(vo[3]), "s"(xg1), "s"(l1) : "memory", "scc");
        if (__any(mx4(xsb) == POISON)) {
          unsigned n = 0;
          do {
            asm volatile("" ::: "memory");
            xsb = ld128(rs, xt1 + t * 4096 + (rr * 128 + 8 * gg) * 2);
          } while (__any(mx4(xsb) == POISON) && ++n < SPIN_CAP);
          if (lane == 0) *(LAS unsigned*)(lds + L_FLAG + 4) = 1u;
        }
      } else {
        v4u xs, x0c[4];
        const int co = xt1 + (t >> 4) * 4096 + rr * 256 + (t & 15) * 16;
        unsigned n = 0;
        for (;;) {
          asm volatile("" ::: "memory");
#pragma unroll
          for (int i = 0; i < 4; ++i) x0c[i] = ld128s(rs, co, i * (16 * 4096));
          xs = ld128(rs, xt1 + t * 4096 + (rr * 128 + 8 * gg) * 2);
          if (!__any(max(max(mx4(x0c[0]), mx4(x0c[1])), max(max(mx4(x0c[2]), mx4(x0c[3])), mx4(xs))) == POISON) || n >= SPIN_CAP) break;
          ++n;
        }
        RSTAMP(8 + 18, n);
        STAMP(8 + 4);
#pragma unroll
        for (int i = 0; i < 4; ++i) {
          const int kk = 16 * i + (t >> 4), f = (kk & 3) | (((kk >> 3) & 1) << 2);
          *(LAS v4u*)(lds + L_RING + kk * 256 + (((t & 15) ^ (f << 1)) * 16)) = x0c[i];
        }
        *(LAS v4u*)(lds + L_XSA + t * 16) = xs;
        if (n != 0 && lane == 0) *(LAS unsigned*)(lds + L_FLAG + 4) = 1u;
      }
    }
    if (wv0 < 4) {
      FRESH();
      f4 aq[4], ak[4], av[4];
      const int qq = l15 >> 2, f = qq | ((kq & 1) << 2);
      lptr pw = lds + L_W + (8 * kq + qq) * 64 + ((hh ^ (kq & 1)) * 32) + (lane & 3) * 8;
      lptr px[4];
#pragma unroll
      for (int u = 0; u < 4; ++u) px[u] = lds + L_RING + (8 * kq + qq) * 256 + (((4 * ph + u) ^ f) * 32) + (lane & 3) * 8;
      v4u fw[2][3], fx[2][4];
#define QKV_LOAD(buf, s) do { \
        fw[buf][0] = ldstr2(pw + (s) * 2048, 256); fw[buf][1] = ldstr2(pw + 32768 + (s) * 2048, 256); fw[buf][2] = ldstr2(pw + 65536 + (s) * 2048, 256); \
        _Pragma("unroll") for (int u = 0; u < 4; ++u) fx[buf][u] = ldstr2(px[u] + (((s) >> 1) % 3) * 16384 + ((s) & 1) * 8192, 1024); } while (0)
#define QKV_MMA(buf) do { \
        _Pragma("unroll") for (int u = 0; u < 4; ++u) { ak[u] = mfma_bf(fw[buf][1], fx[buf][u], ak[u]); av[u] = mfma_bf(fx[buf][u], fw[buf][2], av[u]); aq[u] = mfma_bf(fw[buf][0], fx[buf][u], aq[u]); } \
        _Pragma("unroll") for (int i = 0; i < 7; ++i) { __builtin_amdgcn_sched_group_barrier(0x008, 1, 0); __builtin_amdgcn_sched_group_barrier(0x100, 2, 0); } \
        __builtin_amdgcn_sched_group_barrier(0x008, 5, 0); } while (0)
      const int wo_off = WS_WOF + (cb * 8 + wv) * 16384 + lane * 16;
      auto gemm = [&]() {
        QKV_LOAD(0, 0);
        SCHED();
#pragma unroll
        for (int c = 0; c < 8; ++c) {
          QKV_LOAD(1, 2 * c + 1);
          wof[2 * c] = ld128s(rs, wo_off, 2 * c * 1024);
          QKV_MMA(0);
          SCHED();
          if (c < 7) {
            BAR();
            QKV_LOAD(0, 2 * c + 2);
          }
          wof[2 * c + 1] = ld128s(rs, wo_off, (2 * c + 1) * 1024);
          QKV_MMA(1);
          SCHED();
        }
      };
      for (unsigned attempt = 0;; ++attempt) {
#pragma unroll
        for (int u = 0; u < 4; ++u) { aq[u] = f4{0.f, 0.f, 0.f, 0.f}; ak[u] = aq[u]; av[u] = aq[u]; }
        BAR();
        STAMP(8 + 5);
        gemm();
        STAMP(8 + 6);
#pragma unroll
        for (int u = 0; u < 4; ++u) *(LAS f4*)(lds + L_QRAW + ((wv * 4 + u) * 64 + lane) * 16) = aq[u];
        if (attempt == 0 && __any(ak[0][0] != ak[0][0] || ak[1][0] != ak[1][0] || ak[2][0] != ak[2][0] || ak[3][0] != ak[3][0]) && lane == 0) *(LAS unsigned*)(lds + L_FLAG) = 1u;
        BAR();
        if (*(LAS unsigned*)(lds + L_FLAG) == 0u || attempt >= 4096) break;
        BAR();
      }
      __builtin_amdgcn_s_setprio(2);
      const f4 csk = ldsf4(lds + L_MISC + (32 + hh * 16 + 4 * kq) * 4);
      const v2u wphiT = lds64(lds + L_MISC + 512 + hh * 512 + lane * 8);
      const float csv = *(LAS float*)(lds + L_MISC + (64 + hh * 16 + l15) * 4);
      const v2u ident = v2u{(l15 == 4 * kq ? 0x3F80u : 0u) | (l15 == 4 * kq + 1 ? 0x3F800000u : 0u), (l15 == 4 * kq + 2 ? 0x3F80u : 0u) | (l15 == 4 * kq + 3 ? 0x3F800000u : 0u)};
      const v2u ones2 = v2u{0x3F803F80u, 0x3F803F80u};
      const f4 zero = zero4();
      f4 pk[4];
      v2u vb[4];
#pragma unroll
      for (int u = 0; u < 4; ++u) {
        lptr ps = lds + L_WST + (64 * ph + 16 * u) * 4;
        const float mk = *(LAS float*)(ps + l15 * 4), rk = *(LAS float*)(ps + 512 + l15 * 4);
        const f4 mv = ldsf4(ps + kq * 16), rv = ldsf4(ps + 512 + kq * 16);
        const f4 kf = (ak[u] - mk * csk) * rk;
        pk[u] = mfma16(wphiT, v2u{pkbf(kf[0], kf[1]), pkbf(kf[2], kf[3])}, zero);
        const f4 vf = (av[u] - mv * csv) * rv;
        vb[u] = v2u{pkbf(vf[0], vf[1]), pkbf(vf[2], vf[3])};
      }
      v2u p1[4], p2[4];
      feature_map(pk, p1, p2);
      f4 t1[4], t2[4];
#pragma unroll
      for (int u = 0; u < 4; ++u) { t1[u] = mfma16(p1[u], ident, zero); t2[u] = mfma16(p2[u], ident, zero); }
      f4 kv1 = zero, kv2 = zero, z1 = zero, z2 = zero;
#pragma unroll
      for (int u = 0; u < 4; ++u) {
        const v2u a1 = v2u{pkbf(t1[u][0], t1[u][1]), pkbf(t1[u][2], t1[u][3])}, a2 = v2u{pkbf(t2[u][0], t2[u][1]), pkbf(t2[u][2], t2[u][3])};
        kv1 = mfma16(a1, vb[u], kv1);
        kv2 = mfma16(a2, vb[u], kv2);
        z1 = mfma16(a1, ones2, z1);
        z2 = mfma16(a2, ones2, z2);
      }
      __builtin_amdgcn_s_setprio(0);
      STAMP(8 + 7);
      {
        lptr p = lds + L_KVX + ((ph * 2 + hh) * 2) * 1024 + lane * 16;
        *(LAS f4*)(p) = kv1;
        *(LAS f4*)(p + 1024) = kv2;
        if (l15 == 0) {
          lptr pz = lds + L_KVX + 8192 + ((ph * 2 + hh) * 2) * 64 + kq * 16;
          *(LAS f4*)(pz) = z1;
          *(LAS f4*)(pz + 64) = z2;
        }
      }
      if (layer == 0) DRAIN();
      BAR();
      {
        lptr p = lds + L_KVX + (hh * 2 + ph) * 1024 + lane * 16;
        const f4 s = ldsf4(p) + ldsf4(p + 4096);
        const int ko = WS_KVP + layer * KVP_LAYER + (gg * 16 + rr) * KVP_REC;
        st64(rs, ko + (hh * 2 + ph) * 512 + lane * 8, v2u{san(pkbf(s[0], s[1])), san(pkbf(s[2], s[3]))});
        if (lane < 4) {
          lptr pz = lds + L_KVX + 8192 + (hh * 2 + ph) * 64 + lane * 16;
          const f4 zz = ldsf4(pz) + ldsf4(pz + 256);
          st64(rs, ko + 2048 + (hh * 2 + ph) * 32 + lane * 8, v2u{san(pkbf(zz[0], zz[1])), san(pkbf(zz[2], zz[3]))});
        }
      }
    } else {
      FRESH();
      const int wb = wv - 4;
      const char* xg = uniform_ptr(ws + xt1 + rr * 256);
      unsigned vo[4];
#pragma unroll
      for (int i = 0; i < 4; ++i) { const int kk = 4 * (wb * 4 + i) + kq, f = (kk & 3) | (((kk >> 3) & 1) << 2); vo[i] = kk * 4096 + ((l15 ^ (f << 1)) * 16); }
      auto issue = [&](int c) {
#pragma unroll
        for (int i = 0; i < 4; ++i) dma16s_sc1(xg + (size_t)c * (64 * 4096), vo[i], (unsigned)(L_RING + (c % 3) * 16384 + (wb * 4 + i) * 1024));
      };
      const int wo_off = WS_WOF + (cb * 8 + wv) * 16384 + lane * 16;
      for (unsigned attempt = 0;; ++attempt) {
        unsigned vm = 0;
        if (attempt != 0) { issue(0); issue(1); }
        {
          constexpr int LW = 2;
          float sv[8] = {0.f, 0.f, 0.f, 0.f, 0.f, 0.f, 0.f, 0.f};
          SFor<0, 8>::run([&](auto icc) {
            constexpr int c = decltype(icc)::value;
            constexpr int YW = c == 7 ? 0 : 4 + (c >= 2 ? LW : 0) + (c >= 1 ? LW : 0);
            if (c == 7) asm volatile("s_waitcnt vmcnt(%0)" :: "i"(3 * LW) : "memory");
            else if (c == 0 && attempt == 0) { }
            else if ((c == 3 || c == 4) && wb == 3 && attempt == 0) asm volatile("s_waitcnt vmcnt(%0)" :: "i"(YW + 1) : "memory");
            else asm volatile("s_waitcnt vmcnt(%0)" :: "i"(YW) : "memory");
            BAR();
            if (c == 0 && attempt == 0 && *(LAS unsigned*)(lds + L_FLAG + 4) != 0u) { DRAIN(); issue(1); }
            if (c + 2 < 8) issue(c + 2);
            if (c == 0 && attempt == 0) {
              const v4u xa = lds128(lds + L_XSA + (t - 256) * 16);
#pragma unroll
              for (int p = 0; p < 4; ++p) {
                const unsigned d = xsb[p], e = xa[p];
                const float a = bflo(d), b = bfhi(d), a2 = bflo(e), b2_ = bfhi(e);
                auto r0 = __builtin_amdgcn_permlane32_swap(__float_as_uint(a + a2), __float_as_uint(a * a + a2 * a2), false, false);
                auto r1 = __builtin_amdgcn_permlane32_swap(__float_as_uint(b + b2_), __float_as_uint(b * b + b2_ * b2_), false, false);
                sv[2 * p] = __uint_as_float(r0[0]) + __uint_as_float(r0[1]);
                sv[2 * p + 1] = __uint_as_float(r1[0]) + __uint_as_float(r1[1]);
              }
              asm volatile("" : "+v"(sv[0]), "+v"(sv[1]), "+v"(sv[2]), "+v"(sv[3]), "+v"(sv[4]), "+v"(sv[5]), "+v"(sv[6]), "+v"(sv[7]));
            }
            if (c == 1 && attempt == 0) {
              f4 part;
#pragma unroll
              for (int p = 0; p < 4; ++p) {
                auto r = __builtin_amdgcn_permlane16_swap(__float_as_uint(sv[2 * p]), __float_as_uint(sv[2 * p + 1]), false, false);
                part[p] = row16add(__uint_as_float(r[0]) + __uint_as_float(r[1]));
              }
              if (l15 == 0) *(LAS f4*)(lds + L_STP + wb * 64 + kq * 16) = part;
            }
            if (c == 2 && wb == 3 && attempt == 0) {
              float S1 = 0.f, S2 = 0.f;
              const int p = lane & 7;
#pragma unroll
              for (int b = 0; b < 4; ++b) { S1 += *(LAS float*)(lds + L_STP + b * 64 + (p & 1) * 16 + (p >> 1) * 4); S2 += *(LAS float*)(lds + L_STP + b * 64 + 32 + (p & 1) * 16 + (p >> 1) * 4); }
              const float mean = S1 * (1.f / 512.f);
              const unsigned ms = san(pkbf(mean, ln_sd(S2 * (1.f / 512.f) - mean * mean)));
              if (lane < 8) st32(rs, WS_SX + layer * 8192 + rr * 512 + (8 * gg + lane) * 4, ms);
            }
            if (c < 6) {
              wof[2 * c] = ld128s(rs, wo_off, 2 * c * 1024);
              wof[2 * c + 1] = ld128s(rs, wo_off, (2 * c + 1) * 1024);
            }
            if (attempt != 0) {
#pragma unroll
              for (int i = 0; i < 4; ++i) vm = max(vm, mx4(lds128(lds + L_RING + (c % 3) * 16384 + i * 4096 + (t - 256) * 16)));
            }
            if (c == 6 && wb == 0 && attempt == 0) {
              unsigned sx, n = 0;
              for (;;) {
                asm volatile("" ::: "memory");
                sx = ld32(rs, WS_SX + layer * 8192 + rr * 512 + lane * 4);
                const unsigned sy = ld32(rs, WS_SX + layer * 8192 + rr * 512 + 256 + lane * 4);
                if (!__any(sx == POISON || sy == POISON) || ++n > SPIN_CAP) {
                  *(LAS float*)(lds + L_WST + lane * 4) = bflo(sx);
                  *(LAS float*)(lds + L_WST + 256 + lane * 4) = bflo(sy);
                  *(LAS float*)(lds + L_WST + 512 + lane * 4) = __builtin_amdgcn_rcpf(bfhi(sx));
                  *(LAS float*)(lds + L_WST + 768 + lane * 4) = __builtin_amdgcn_rcpf(bfhi(sy));
                  break;
                }
              }
              RSTAMP(8 + 19, n);
            }
            if (c == 6) {
#pragma unroll
              for (int s = 12; s < 16; ++s) wof[s] = ld128s(rs, wo_off, s * 1024);
            }
          });
        }
        if (__any(vm == POISON) && lane == 0) *(LAS unsigned*)(lds + L_FLAG) = 1u;
        BAR();
        if (*(LAS unsigned*)(lds + L_FLAG) == 0u || attempt >= 4096) break;
        BAR();
        if (t == 256) *(LAS unsigned*)(lds + L_FLAG) = 0u;
      }
      if (layer == 0) DRAIN();
      BAR();
      const f4 csq = ldsf4(lds + L_MISC + (hh * 16 + 4 * kq) * 4), zq = zero4();
      const v2u wphiT = lds64(lds + L_MISC + 512 + hh * 512 + lane * 8);
      f4 pq[4];
#pragma unroll
      for (int u = 0; u < 4; ++u) {
        const f4 aq = ldsf4(lds + L_QRAW + ((wb * 4 + u) * 64 + lane) * 16);
        const float mk = *(LAS float*)(lds + L_WST + (64 * ph + 16 * u + l15) * 4), rk = *(LAS float*)(lds + L_WST + 512 + (64 * ph + 16 * u + l15) * 4);
        const f4 qf = (aq - mk * csq) * rk;
        pq[u] = mfma16(wphiT, v2u{pkbf(qf[0], qf[1]), pkbf(qf[2], qf[3])}, zq);
      }
      feature_map(pq, phq1, phq2);
    }
    STAMP(8 + 8);
    unsigned xres[2][2];
    {
      FRESH();
#pragma unroll
      for (int pt = 0; pt < 2; ++pt)
#pragma unroll
        for (int j = 0; j < 2; ++j) {
          const int o = xt1 + (cb * 128 + 16 * wv + 4 * kq + 2 * j) * 4096 + (rb * 32 + 16 * pt + l15) * 2;
          xres[pt][j] = (unsigned)__builtin_amdgcn_raw_buffer_load_b16(rs, o, 0, SC1) | ((unsigned)__builtin_amdgcn_raw_buffer_load_b16(rs, o + 4096, 0, SC1) << 16);
        }
    }

    {
      FRESH();
      if (wv < 4) __builtin_amdgcn_s_sleep(PRE3);
      const int grp = t >> 7, e = t & 127;
      const int ko = WS_KVP + layer * KVP_LAYER + (gg * 16 + 4 * grp) * KVP_REC;
      const int zo = e < 8 ? 2048 + e * 16 : e * 16;
      const bool zw = (wv & 1) == 0;
      v4u pv[4], pz[4];
      unsigned n = 0;
      for (;;) {
        asm volatile("" ::: "memory");
        unsigned mm = 0;
#pragma unroll
        for (int i = 0; i < 4; ++i) pv[i] = ld128s(rs, ko + e * 16, i * KVP_REC);
        if (zw) {
#pragma unroll
          for (int i = 0; i < 4; ++i) pz[i] = ld128s(rs, ko + zo, i * KVP_REC);
#pragma unroll
          for (int i = 0; i < 4; ++i) mm = max(mm, mx4(pz[i]));
        }
#pragma unroll
        for (int i = 0; i < 4; ++i) mm = max(mm, mx4(pv[i]));
        if (!__any(mm == POISON) || ++n > SPIN_CAP) break;
      }
      RSTAMP(8 + 20, n);
      STAMP(8 + 9);
      f2 a[4];
#pragma unroll
      for (int j = 0; j < 4; ++j) {
        a[j] = f2{0.f, 0.f};
#pragma unroll
        for (int i = 0; i < 4; ++i) { const unsigned d = pv[i][j]; a[j] += f2{bflo(d), bfhi(d)}; }
      }
      *(LAS f4*)(lds + L_RED + (grp * 136 + e) * 32) = f4{a[0].x, a[0].y, a[1].x, a[1].y};
      *(LAS f4*)(lds + L_RED + (grp * 136 + e) * 32 + 16) = f4{a[2].x, a[2].y, a[3].x, a[3].y};
      if (zw) {
#pragma unroll
        for (int j = 0; j < 4; ++j) {
          a[j] = f2{0.f, 0.f};
#pragma unroll
          for (int i = 0; i < 4; ++i) { const unsigned d = pz[i][j]; a[j] += f2{bflo(d), bfhi(d)}; }
        }
        if (e < 8) {
          *(LAS f4*)(lds + L_RED + (grp * 136 + 128 + e) * 32) = f4{a[0].x, a[0].y, a[1].x, a[1].y};
          *(LAS f4*)(lds + L_RED + (grp * 136 + 128 + e) * 32 + 16) = f4{a[2].x, a[2].y, a[3].x, a[3].y};
        }
      }
    }
    BAR();
    STAMP(8 + 10);
    if (wv0 >= 4) {
      FRESH();
      lptr p = lds + L_RED + hh * 2048 + lane * 16, pz = lds + L_RED + 128 * 32 + hh * 128 + kq * 16;
      f4 k1 = ldsf4(p), k2 = ldsf4(p + 1024), z1 = ldsf4(pz), z2 = ldsf4(pz + 64);
#pragma unroll
      for (int g = 1; g < 4; ++g) { k1 += ldsf4(p + g * 136 * 32); k2 += ldsf4(p + 1024 + g * 136 * 32); z1 += ldsf4(pz + g * 136 * 32); z2 += ldsf4(pz + 64 + g * 136 * 32); }
      const v2u k1b = v2u{pkbf(k1[0], k1[1]), pkbf(k1[2], k1[3])}, k2b = v2u{pkbf(k2[0], k2[1]), pkbf(k2[2], k2[3])};
      const v2u z1b = v2u{pkbf(z1[0], z1[1]), pkbf(z1[2], z1[3])}, z2b = v2u{pkbf(z2[0], z2[1]), pkbf(z2[2], z2[3])};
      const f4 zero = zero4();
      f4 num[4], den[4];
#pragma unroll
      for (int u = 0; u < 4; ++u) { num[u] = mfma16(k1b, phq1[u], zero); den[u] = mfma16(z1b, phq1[u], zero); }
#pragma unroll
      for (int u = 0; u < 4; ++u) { num[u] = mfma16(k2b, phq2[u], num[u]); den[u] = mfma16(z2b, phq2[u], den[u]); }
      v2u ob[4];
#pragma unroll
      for (int u = 0; u < 4; ++u) {
        const float ri = __builtin_amdgcn_rcpf(rbf(rbf(den[u][0]) + 1e-6f));
        const unsigned n01 = pkbf(num[u][0], num[u][1]), n23 = pkbf(num[u][2], num[u][3]);
        ob[u] = v2u{san(pkbf(bflo(n01) * ri, bfhi(n01) * ri)), san(pkbf(bflo(n23) * ri, bfhi(n23) * ri))};
      }
#pragma unroll
      for (int u = 0; u < 4; u += 2) {
        auto r0 = __builtin_amdgcn_permlane16_swap(ob[u].x, ob[u + 1].x, false, false);
        auto r1 = __builtin_amdgcn_permlane16_swap(ob[u].y, ob[u + 1].y, false, false);
        const v4u o = v4u{r0[0], r1[0], r0[1], r1[1]};
        const int tile = rr * 8 + 4 * ph + u + (kq & 1);
        st128(rs, WS_OB + layer * XT_BYTES + (tile * 16 + gg) * 1024 + ((2 * hh + (kq >> 1)) * 16 + l15) * 16, o);
      }
    }
    BAR();
    STAMP(8 + 11);

    {
      FRESH();
      __builtin_amdgcn_s_sleep(PRE4);
      const int oo = WS_OB + layer * XT_BYTES + rb * 32768 + t * 16;
      v4u ov[4];
      unsigned n = 0;
      for (;;) {
        asm volatile("" ::: "memory");
#pragma unroll
        for (int i = 0; i < 4; ++i) ov[i] = ld128s(rs, oo, i * 8192);
        if (!__any(max(max(mx4(ov[0]), mx4(ov[1])), max(mx4(ov[2]), mx4(ov[3]))) == POISON) || ++n > SPIN_CAP) break;
      }
      STAMP(8 + 12);
#pragma unroll
      for (int i = 0; i < 4; ++i) *(LAS v4u*)(lds + L_OT + t * 16 + i * 8192) = ov[i];
    }
    BAR();
    STAMP(8 + 13);
    {
      FRESH();
      f4 oa[2] = {f4{0.f, 0.f, 0.f, 0.f}, f4{0.f, 0.f, 0.f, 0.f}};
      lptr po = lds + L_OT + lane * 16;
#pragma unroll
      for (int s0 = 0; s0 < 16; s0 += 8) {
        v4u fo[8][2];
#pragma unroll
        for (int s = 0; s < 8; ++s) { fo[s][0] = lds128(po + (s0 + s) * 1024); fo[s][1] = lds128(po + 16384 + (s0 + s) * 1024); }
        SCHED();
#pragma unroll
        for (int s = 0; s < 8; ++s) { oa[0] = mfma_bf(wof[s0 + s], fo[s][0], oa[0]); oa[1] = mfma_bf(wof[s0 + s], fo[s][1], oa[1]); }
        SCHED();
      }
      unsigned xo[2][2];
#pragma unroll
      for (int pt = 0; pt < 2; ++pt)
#pragma unroll
        for (int j = 0; j < 2; ++j) {
          const unsigned a = pkbf(oa[pt][2 * j], oa[pt][2 * j + 1]);
          xo[pt][j] = pkbf(bflo(xres[pt][j]) + bflo(a), bfhi(xres[pt][j]) + bfhi(a));
        }
      if (layer == 0) {
#pragma unroll
        for (int pt = 0; pt < 2; ++pt)
#pragma unroll
          for (int j = 0; j < 2; ++j) *(LAS unsigned*)(lds + L_XTILE + (8 * wv + 2 * kq + j) * 144 + (16 * pt + l15) * 4) = san(xo[pt][j]);
        BAR();
        STAMP(8 + 14);
        lptr xt = lds + L_XTILE;
        {
          const int pair = t >> 3, quad = t & 7;
          st128(rs, WS_XT + 2 * XT_BYTES + (cb * 64 + pair) * 8192 + (rb * 32 + quad * 4) * 4, lds128(xt + pair * 144 + quad * 16));
        }
        const v4u e = lds128(xt + lane * 144 + wv * 16), pc = lds128(xt + wv * 16);
        float s1[4], s2[4], c[4];
#pragma unroll
        for (int p = 0; p < 4; ++p) {
          const unsigned ep_ = e[p], pp = pc[p];
          c[p] = bflo(pp);
          const float a = bflo(ep_) - c[p], b = bfhi(ep_) - c[p];
          s1[p] = a + b;
          s2[p] = a * a + b * b;
        }
        float bb[4];
#pragma unroll
        for (int p = 0; p < 4; ++p) {
          auto r = __builtin_amdgcn_permlane32_swap(__float_as_uint(s1[p]), __float_as_uint(s2[p]), false, false);
          bb[p] = __uint_as_float(r[0]) + __uint_as_float(r[1]);
        }
        float cc[2];
#pragma unroll
        for (int p = 0; p < 2; ++p) {
          auto r = __builtin_amdgcn_permlane16_swap(__float_as_uint(bb[2 * p]), __float_as_uint(bb[2 * p + 1]), false, false);
          cc[p] = row16add(__uint_as_float(r[0]) + __uint_as_float(r[1]));
        }
        auto r = __builtin_amdgcn_permlane32_swap(__float_as_uint(cc[0]), __float_as_uint(cc[1]), false, false);
        const float S1 = __uint_as_float(r[0]), S2 = __uint_as_float(r[1]);
        const float cp = kq == 0 ? c[0] : kq == 1 ? c[1] : kq == 2 ? c[2] : c[3];
        const float mean = cp + S1 * (1.f / 128.f), m2 = S2 - S1 * S1 * (1.f / 128.f);
        if (l15 == 0) st32(rs, WS_ST + (cb * 2048 + rb * 32 + 4 * wv + kq) * 4, san(pkbf(mean, m2 * (1.f / 128.f))));
        STAMP(8 + 15);
      } else {
#pragma unroll
        for (int pt = 0; pt < 2; ++pt) *(LAS v2u*)(lds + L_XTILE + (16 * pt + l15) * 272 + wv * 32 + kq * 8) = v2u{xo[pt][0], xo[pt][1]};
        BAR();
        const int pos = t >> 4, chunk = t & 15;
        const v4u d = lds128(lds + L_XTILE + pos * 272 + chunk * 16);
        __builtin_amdgcn_raw_buffer_store_b128(d, __builtin_amdgcn_make_buffer_rsrc(out, 0, 2048 * 512 * 2, 0x00020000), ((rb * 32 + pos) * 512 + cb * 128 + chunk * 8) * 2, 0, SC1);
        STAMP(8 + 15);
        STAMP0(61);
        CSTAMP(62);
      }
      BAR();
    }
    if (layer == 0) {
      { FRESH(); build_copies(lds, t); }
      FRESH();
      float m[4], rinv[4];
      {
        __builtin_amdgcn_s_sleep(PRE5);
        const int xoff = WS_XT + 2 * XT_BYTES + pme * 8192 + t * 16;
        const int soff = WS_ST + t * 16;
        v4u sa[4];
        unsigned n = 0;
        for (;;) {
          asm volatile("" ::: "memory");
          xr = ld128(rs, xoff);
#pragma unroll
          for (int c = 0; c < 4; ++c) sa[c] = ld128s(rs, soff, c * 8192);
          if (!__any(max(max(mx4(xr), mx4(sa[0])), max(max(mx4(sa[1]), mx4(sa[2])), mx4(sa[3]))) == POISON) || ++n > SPIN_CAP) break;
        }
        RSTAMP(8 + 24 + 16, n);
        {
          float mean[4], var[4];
#pragma unroll
          for (int p = 0; p < 4; ++p) {
            const unsigned q0 = sa[0][p], q1 = sa[1][p], q2 = sa[2][p], q3 = sa[3][p];
            mean[p] = 0.25f * ((bflo(q0) + bflo(q1)) + (bflo(q2) + bflo(q3)));
            const float e0 = bflo(q0) - mean[p], e1 = bflo(q1) - mean[p], e2 = bflo(q2) - mean[p], e3 = bflo(q3) - mean[p];
            var[p] = 0.25f * (((bfhi(q0) + bfhi(q1)) + (bfhi(q2) + bfhi(q3))) + ((e0 * e0 + e1 * e1) + (e2 * e2 + e3 * e3)));
          }
          SCHED();
#pragma unroll
          for (int p = 0; p < 4; ++p) { m[p] = rbf(mean[p]); var[p] = rbf(fmaxf(var[p], 0.f)) + 1e-5f; }
          SCHED();
#pragma unroll
          for (int p = 0; p < 4; ++p) var[p] = __builtin_amdgcn_sqrtf(rbf(var[p]));
          SCHED();
#pragma unroll
          for (int p = 0; p < 4; ++p) rinv[p] = __builtin_amdgcn_rcpf(rbf(var[p]));
        }
      }
      STAMP(8 + 24 + 0);
      conv_prep(m, rinv);
    } else {
      unsigned z = 0u;
      asm volatile("" : "+v"(z));
      xr = v4u{z, z, z, z};
      tD = xr;
    }
  }
}

extern "C" void kernel_launch(void* const* d_in, const int* in_sizes, int n_in, void* d_out, int out_size, void* d_ws, size_t ws_size,
                              hipStream_t stream) {
  (void)in_sizes; (void)n_in; (void)out_size; (void)ws_size;
  (void)hipFuncSetAttribute((const void*)interleave_layers_kernel, hipFuncAttributeMaxDynamicSharedMemorySize, LDS_BYTES);
  interleave_layers_kernel<<<256, 512, LDS_BYTES, stream>>>(
      (const unsigned short*)d_in[0], (const unsigned short*)d_in[1], (const unsigned short*)d_in[2], (const unsigned short*)d_in[3],
      (const unsigned short*)d_in[4], (const unsigned short*)d_in[5], (const unsigned short*)d_in[6], (const unsigned short*)d_in[8],
      (const unsigned short*)d_in[9], (const unsigned short*)d_in[10], (unsigned short*)d_out, (char*)d_ws);
}
```
